```python
import torch

def _softmax(x, axis=-1):
    return torch.softmax(x, dim=axis)

def _gelu(x):
    return torch.nn.functional.gelu(x, approximate='tanh')

def _silu(x):
    return torch.nn.functional.silu(x)

def _sigmoid(x):
    return torch.sigmoid(x)

def _layernorm(x, axis=-1, eps=1e-05):
    m = x.mean(dim=axis, keepdim=True)
    v = x.var(dim=axis, keepdim=True, unbiased=False)
    return (x - m) / torch.sqrt(v + eps)

def _scatter_add(tgt, idx, src, axis=0):
    return tgt.clone().index_add_(axis, idx, src)

def _segment_sum(src, seg, n):
    out = torch.zeros((n,) + src.shape[1:], dtype=src.dtype)
    return out.index_add_(0, seg, src)

def _pad(x, pw):
    flat = tuple((v for pair in reversed(pw) for v in pair))
    return torch.nn.functional.pad(x, flat)
kernel_name = 'adaLN_cond__memory_aug_recurrent_mem_tr__glu_gated_reglu__prompt'

def setup_inputs():

    _w = lambda a: (lambda t: t.to(torch.bfloat16) if t.is_floating_point() else t)(torch.as_tensor(a))

    def _A_setup():
        B, S, Dm = (1, 2048, 576)
        H, Dh = (32, 128)
        M, seg = (8, 32)
        seg = min(seg, S)
        S = S // seg * seg
        return {'x': _w((lambda *s: torch.randn(*s, dtype=torch.bfloat16) * 0.05)(B, S, Dm)), 'mem0': _w((lambda *s: torch.randn(*s, dtype=torch.bfloat16) * 0.8)(B, M, Dm)), 'Wq': _w((lambda *s: torch.randn(*s, dtype=torch.bfloat16) * 0.8)(Dm, H, Dh) * Dm ** (-0.5)), 'Wk': _w((lambda *s: torch.randn(*s, dtype=torch.bfloat16) * 0.8)(Dm, H, Dh) * Dm ** (-0.5)), 'Wv': _w((lambda *s: torch.randn(*s, dtype=torch.bfloat16) * 0.8)(Dm, H, Dh) * Dm ** (-0.5)), 'Wo': _w((lambda *s: torch.randn(*s, dtype=torch.bfloat16) * 0.8)(H, Dh, Dm) * (H * Dh) ** (-0.5)), 'seg_len': seg}
    _A_in = _A_setup()

    def _B_setup():
        B, S, D, F = (1, 2048, 576, 4608)
        return {'x': _w((lambda *s: torch.randn(*s, dtype=torch.bfloat16) * 0.05)(B, S, D)), 'W_gate': _w((lambda *s: torch.randn(*s, dtype=torch.bfloat16) * 0.4)(D, F) * (2.0 / D) ** 0.5), 'W_up': _w((lambda *s: torch.randn(*s, dtype=torch.bfloat16) * 0.4)(D, F) * (2.0 / D) ** 0.5), 'W_down': _w((lambda *s: torch.randn(*s, dtype=torch.bfloat16) * 0.4)(F, D) * (1.0 / F) ** 0.5)}
    _B_in = _B_setup()
    return {'A_x': _A_in['x'], 'A_mem0': _A_in['mem0'], 'A_Wq': _A_in['Wq'], 'A_Wk': _A_in['Wk'], 'A_Wv': _A_in['Wv'], 'A_Wo': _A_in['Wo'], 'A_seg_len': _A_in['seg_len'], 'B_x': _B_in['x'], 'B_W_gate': _B_in['W_gate'], 'B_W_up': _B_in['W_up'], 'B_W_down': _B_in['W_down'], 'c': _w((lambda *s: torch.randn(*s, dtype=torch.bfloat16) * 0.1)(1, 576) * 8.0), 'w_mod': _w((lambda *s: torch.randn(*s, dtype=torch.bfloat16) * 0.1)(576, 6 * 576) * 576 ** (-0.5) * 8.0)}

def reference(A_x, A_mem0, A_Wq, A_Wk, A_Wv, A_Wo, A_seg_len, B_x, B_W_gate, B_W_up, B_W_down, c, w_mod):

    def _A_ref(x, mem0, Wq, Wk, Wv, Wo, seg_len):
        B, S, Dm = x.shape
        H, Dh = (Wq.shape[1], Wq.shape[2])
        M = mem0.shape[1]
        scale = Dh ** (-0.5)
        n_seg = S // seg_len
        L = M + seg_len
        idx = torch.arange(L)
        mask = (idx[:, None] < M) | (idx[:, None] >= idx[None, :])
        mem = mem0
        outs = []
        for s in range(n_seg):
            xs = x[:, s * seg_len:(s + 1) * seg_len, :].clone()
            h = torch.concatenate([mem, xs], axis=1)
            q = torch.einsum('btd,dhe->bhte', h, Wq)
            k = torch.einsum('btd,dhe->bhte', h, Wk)
            v = torch.einsum('btd,dhe->bhte', h, Wv)
            scores = torch.einsum('bhqe,bhke->bhqk', q, k) * scale
            scores = torch.where(mask[None, None], scores, -1000000000.0)
            p = _softmax(scores, axis=-1)
            o = torch.einsum('bhqk,bhke->bhqe', p, v)
            o = torch.einsum('bhte,hed->btd', o, Wo)
            mem = o[:, :M, :].clone()
            outs.append(o[:, M:, :])
        return torch.concatenate(outs, axis=1)

    def _B_ref(x, W_gate, W_up, W_down):
        gate = torch.einsum('bsd,df->bsf', x, W_gate)
        up = torch.einsum('bsd,df->bsf', x, W_up)
        gate = gate * (lambda x, dt: x.to(dt))(gate > 0, gate.dtype)
        h = gate * up
        return torch.einsum('bsf,fd->bsd', h, W_down)
    x = A_x
    mod = (_silu(c) @ w_mod).reshape(c.shape[0], 6, 1, -1)
    sa, ca, ga, sb, cb, gb = (mod[:, 0].clone(), mod[:, 1].clone(), mod[:, 2].clone(), mod[:, 3].clone(), mod[:, 4].clone(), mod[:, 5].clone())
    h = _layernorm(x, axis=-1) * (1 + ca) + sa
    x = x + ga * _A_ref(h, A_mem0, A_Wq, A_Wk, A_Wv, A_Wo, A_seg_len)
    h = _layernorm(x, axis=-1) * (1 + cb) + sb
    x = x + gb * _B_ref(h, B_W_gate, B_W_up, B_W_down)
    return x
```

```cpp
#include <hip/hip_runtime.h>
#include <hip/hip_bf16.h>
#include <stdint.h>

typedef __attribute__((ext_vector_type(8))) short bf16x8;
typedef __attribute__((ext_vector_type(4))) short s16x4;
typedef __attribute__((ext_vector_type(4))) float f32x4;
typedef unsigned short u16;
typedef unsigned long long u64;

#define GAS __attribute__((address_space(1)))
#define LAS __attribute__((address_space(3)))
#define RLX_AGENT __ATOMIC_RELAXED, __HIP_MEMORY_SCOPE_AGENT

constexpr int S_ = 2048, DM = 576, NH = 32, DH = 128, HD = NH * DH;
constexpr int MEM = 8, SEG = 32, NSEG = 64, FF = 4608;
constexpr int QKV_LD = 3 * HD;

__device__ __forceinline__ float bf2f(u16 v) { return __uint_as_float(((unsigned)v) << 16); }
__device__ __forceinline__ u16 f2bf(float f) {
  __hip_bfloat16 b = __float2bfloat16(f);
  return *reinterpret_cast<u16*>(&b);
}
__device__ __forceinline__ unsigned pack2(float a, float b) { return (unsigned)f2bf(a) | ((unsigned)f2bf(b) << 16); }

__global__ void k_mod(const u16* __restrict__ c, const u16* __restrict__ wmod, float* __restrict__ mod) {
  __shared__ float red[4][64];
  int col = blockIdx.x * 64 + (threadIdx.x & 63);
  int ks = threadIdx.x >> 6;
  float acc = 0.f;
  for (int k = ks * 144; k < ks * 144 + 144; ++k) {
    float cv = bf2f(c[k]);
    float sv = cv / (1.f + __expf(-cv));
    acc += sv * bf2f(wmod[(size_t)k * (6 * DM) + col]);
  }
  red[ks][threadIdx.x & 63] = acc;
  __syncthreads();
  if (ks == 0) mod[col] = red[0][threadIdx.x] + red[1][threadIdx.x] + red[2][threadIdx.x] + red[3][threadIdx.x];
}

template <bool XF32>
__global__ void k_ln(const void* __restrict__ xin, const float* __restrict__ shift, const float* __restrict__ scale,
                     u16* __restrict__ h) {
  int row = blockIdx.x * 4 + (threadIdx.x >> 6);
  int lane = threadIdx.x & 63;
  float v[9];
  float s = 0.f;
#pragma unroll
  for (int i = 0; i < 9; ++i) {
    int d = lane + i * 64;
    if (XF32) v[i] = ((const float*)xin)[(size_t)row * DM + d];
    else v[i] = bf2f(((const u16*)xin)[(size_t)row * DM + d]);
    s += v[i];
  }
#pragma unroll
  for (int o = 32; o > 0; o >>= 1) s += __shfl_xor(s, o);
  float m = s * (1.f / DM);
  float q = 0.f;
#pragma unroll
  for (int i = 0; i < 9; ++i) { float d = v[i] - m; q += d * d; }
#pragma unroll
  for (int o = 32; o > 0; o >>= 1) q += __shfl_xor(q, o);
  float r = rsqrtf(q * (1.f / DM) + 1e-5f);
#pragma unroll
  for (int i = 0; i < 9; ++i) {
    int d = lane + i * 64;
    h[(size_t)row * DM + d] = f2bf((v[i] - m) * r * (1.f + scale[d]) + shift[d]);
  }
}

__device__ __forceinline__ s16x4 tr_read(const u16* p) {
  typedef __attribute__((ext_vector_type(4))) short v4i16_t;
  return __builtin_bit_cast(s16x4, __builtin_amdgcn_ds_read_tr16_b64_v4i16((LAS v4i16_t*)p));
}

template <int BN, int EPI>
__global__ __launch_bounds__(256, 2) void k_gemm(const u16* __restrict__ A, const u16* __restrict__ B,
                                                 const u16* __restrict__ B2, int K, int ldb, int N,
                                                 void* __restrict__ Cout, const void* __restrict__ resid,
                                                 const float* __restrict__ gvec) {
  constexpr int BM = 128, BK = 64;
  constexpr int NB = (EPI == 2) ? 2 : 1;
  constexpr int A_BYTES = BM * BK * 2;
  constexpr int B_BYTES = BK * BN * 2;
  constexpr int STAGE = A_BYTES + NB * B_BYTES;
  __shared__ __attribute__((aligned(16))) char lds[2 * STAGE];
  const int tid = threadIdx.x, lane = tid & 63, wid = tid >> 6;
  const int wr = wid >> 1, wc = wid & 1;
  const int bm = blockIdx.y * BM, bn = blockIdx.x * BN;
  constexpr int WN = BN / 2;
  constexpr int NT = WN / 16;
  f32x4 acc[NB][4][NT];
#pragma unroll
  for (int b = 0; b < NB; ++b)
#pragma unroll
    for (int i = 0; i < 4; ++i)
#pragma unroll
      for (int j = 0; j < NT; ++j) acc[b][i][j] = f32x4{0.f, 0.f, 0.f, 0.f};

  auto stage = [&](int buf, int kt) {
    char* base = lds + buf * STAGE;
#pragma unroll
    for (int i = 0; i < 4; ++i) {
      int c = tid + i * 256;
      int row = c >> 3, pos = c & 7, ch = pos ^ (row & 7);
      const u16* src = A + (size_t)(bm + row) * K + kt * BK + ch * 8;
      __builtin_amdgcn_global_load_lds((const GAS void*)src, (LAS void*)(base + (i * 256 + wid * 64) * 16), 16, 0, 0);
    }
    constexpr int CPR = BN / 8;
    constexpr int NCH = BK * CPR;
#pragma unroll
    for (int b = 0; b < NB; ++b) {
      const u16* Bm = b ? B2 : B;
#pragma unroll
      for (int i = 0; i < NCH / 256; ++i) {
        int c = tid + i * 256;
        int row = c / CPR, pos = c % CPR;
        int ch = pos ^ ((((row & 3) << 2) | ((row >> 2) & 3)) & (CPR - 1));
        const u16* src = Bm + (size_t)(kt * BK + row) * ldb + bn + ch * 8;
        __builtin_amdgcn_global_load_lds((const GAS void*)src,
                                         (LAS void*)(base + A_BYTES + b * B_BYTES + (i * 256 + wid * 64) * 16), 16, 0, 0);
      }
    }
  };

  const int nkt = K / BK;
  stage(0, 0);
  asm volatile("s_waitcnt vmcnt(0)" ::: "memory");
  __syncthreads();
  for (int kt = 0; kt < nkt; ++kt) {
    int cur = kt & 1;
    if (kt + 1 < nkt) stage(cur ^ 1, kt + 1);
    const char* base = lds + cur * STAGE;
#pragma unroll
    for (int kk = 0; kk < 2; ++kk) {
      bf16x8 af[4];
#pragma unroll
      for (int i = 0; i < 4; ++i) {
        int row = wr * 64 + i * 16 + (lane & 15);
        int ch = (kk * 4 + (lane >> 4)) ^ (row & 7);
        af[i] = *(const bf16x8*)(base + row * 128 + ch * 16);
      }
#pragma unroll
      for (int b = 0; b < NB; ++b) {
#pragma unroll
        for (int j = 0; j < NT; ++j) {
          bf16x8 bfr;
#pragma unroll
          for (int t = 0; t < 2; ++t) {
            int krow = kk * 32 + 8 * (lane >> 4) + 4 * t + ((lane & 15) >> 2);
            int col = wc * WN + j * 16 + 4 * (lane & 3);
            int ch = (col >> 3) ^ ((((krow & 3) << 2) | ((krow >> 2) & 3)) & (BN / 8 - 1));
            const u16* p = (const u16*)(base + A_BYTES + b * B_BYTES + krow * (BN * 2) + ch * 16 + (col & 7) * 2);
            s16x4 v = tr_read(p);
            bfr[4 * t + 0] = v[0]; bfr[4 * t + 1] = v[1]; bfr[4 * t + 2] = v[2]; bfr[4 * t + 3] = v[3];
          }
#pragma unroll
          for (int i = 0; i < 4; ++i)
            acc[b][i][j] = __builtin_amdgcn_mfma_f32_16x16x32_bf16(af[i], bfr, acc[b][i][j], 0, 0, 0);
        }
      }
    }
    asm volatile("s_waitcnt vmcnt(0)" ::: "memory");
    __syncthreads();
  }
#pragma unroll
  for (int i = 0; i < 4; ++i)
#pragma unroll
    for (int j = 0; j < NT; ++j)
#pragma unroll
      for (int r = 0; r < 4; ++r) {
        int row = bm + wr * 64 + i * 16 + (lane >> 4) * 4 + r;
        int col = bn + wc * WN + j * 16 + (lane & 15);
        float v = acc[0][i][j][r];
        if (EPI == 0) {
          ((u16*)Cout)[(size_t)row * N + col] = f2bf(v);
        } else if (EPI == 1) {
          float xv = bf2f(((const u16*)resid)[(size_t)row * DM + col]);
          ((float*)Cout)[(size_t)row * DM + col] = xv + gvec[col] * v;
        } else if (EPI == 2) {
          float g = bf2f(f2bf(v)), u = bf2f(f2bf(acc[NB - 1][i][j][r]));
          g = g > 0.f ? g : 0.f;
          ((u16*)Cout)[(size_t)row * N + col] = f2bf(g * u);
        } else {
          float xv = ((const float*)resid)[(size_t)row * DM + col];
          ((u16*)Cout)[(size_t)row * DM + col] = f2bf(xv + gvec[col] * v);
        }
      }
}


__global__ __launch_bounds__(256) void k_xattn(const u16* __restrict__ qkv, const u16* __restrict__ kvm,
                                               u16* __restrict__ ox) {
  __shared__ __attribute__((aligned(16))) u16 sP[4][32][72];
  __shared__ __attribute__((aligned(16))) u16 sV[4][64][136];
  const int lane = threadIdx.x & 63, wid = threadIdx.x >> 6;
  const int pair = blockIdx.x * 4 + wid;
  const int seg = pair >> 5, h = pair & 31;
  const u16* qbase = qkv + (size_t)(seg * SEG) * QKV_LD + h * DH;
  const u16* kbase = qbase + HD;
  const u16* vbase = qbase + 2 * HD;
  const u16* kmb = kvm + ((size_t)seg * 2 + 0) * MEM * HD + h * DH;
  const u16* vmb = kvm + ((size_t)seg * 2 + 1) * MEM * HD + h * DH;
  for (int i = lane; i < 64 * 16; i += 64) {
    int key = i >> 4, c = (i & 15) * 8;
    bf16x8 v = {0, 0, 0, 0, 0, 0, 0, 0};
    if (key < 8) v = *(const bf16x8*)(vmb + (size_t)key * HD + c);
    else if (key < 40) v = *(const bf16x8*)(vbase + (size_t)(key - 8) * QKV_LD + c);
    *(bf16x8*)&sV[wid][key][c] = v;
  }
  f32x4 sc[2][3];
#pragma unroll
  for (int i = 0; i < 2; ++i)
#pragma unroll
    for (int j = 0; j < 3; ++j) sc[i][j] = f32x4{0, 0, 0, 0};
#pragma unroll
  for (int ks = 0; ks < 4; ++ks) {
    int kd = ks * 32 + (lane >> 4) * 8;
    bf16x8 a[2], b[3];
#pragma unroll
    for (int i = 0; i < 2; ++i) a[i] = *(const bf16x8*)(qbase + (size_t)(i * 16 + (lane & 15)) * QKV_LD + kd);
#pragma unroll
    for (int j = 0; j < 3; ++j) {
      int key = j * 16 + (lane & 15);
      if (key < 8) b[j] = *(const bf16x8*)(kmb + (size_t)key * HD + kd);
      else if (key < 40) b[j] = *(const bf16x8*)(kbase + (size_t)(key - 8) * QKV_LD + kd);
      else b[j] = bf16x8{0, 0, 0, 0, 0, 0, 0, 0};
    }
#pragma unroll
    for (int i = 0; i < 2; ++i)
#pragma unroll
      for (int j = 0; j < 3; ++j) sc[i][j] = __builtin_amdgcn_mfma_f32_16x16x32_bf16(a[i], b[j], sc[i][j], 0, 0, 0);
  }
  const float scale = 0.08838834764831845f;
#pragma unroll
  for (int i = 0; i < 2; ++i)
#pragma unroll
    for (int r = 0; r < 4; ++r) {
      int qrow = i * 16 + (lane >> 4) * 4 + r;
      float v[3];
      float mx = -3e38f;
#pragma unroll
      for (int j = 0; j < 3; ++j) {
        int key = j * 16 + (lane & 15);
        bool ok = (key < 8) || (key < 40 && (key - 8) <= qrow);
        v[j] = ok ? sc[i][j][r] * scale : -3e38f;
        mx = fmaxf(mx, v[j]);
      }
#pragma unroll
      for (int o = 1; o < 16; o <<= 1) mx = fmaxf(mx, __shfl_xor(mx, o));
      float sum = 0.f;
#pragma unroll
      for (int j = 0; j < 3; ++j) { v[j] = v[j] > -1e38f ? __expf(v[j] - mx) : 0.f; sum += v[j]; }
#pragma unroll
      for (int o = 1; o < 16; o <<= 1) sum += __shfl_xor(sum, o);
      float inv = 1.f / sum;
#pragma unroll
      for (int j = 0; j < 3; ++j) sP[wid][qrow][j * 16 + (lane & 15)] = f2bf(v[j] * inv);
      sP[wid][qrow][48 + (lane & 15)] = 0;
    }
  __syncthreads();
#pragma unroll
  for (int nt = 0; nt < 8; ++nt) {
    f32x4 o[2] = {f32x4{0, 0, 0, 0}, f32x4{0, 0, 0, 0}};
#pragma unroll
    for (int ks = 0; ks < 2; ++ks) {
      bf16x8 b;
#pragma unroll
      for (int e = 0; e < 8; ++e) b[e] = (short)sV[wid][ks * 32 + (lane >> 4) * 8 + e][nt * 16 + (lane & 15)];
#pragma unroll
      for (int i = 0; i < 2; ++i) {
        bf16x8 a = *(const bf16x8*)&sP[wid][i * 16 + (lane & 15)][ks * 32 + (lane >> 4) * 8];
        o[i] = __builtin_amdgcn_mfma_f32_16x16x32_bf16(a, b, o[i], 0, 0, 0);
      }
    }
#pragma unroll
    for (int i = 0; i < 2; ++i)
#pragma unroll
      for (int r = 0; r < 4; ++r) {
        int qrow = i * 16 + (lane >> 4) * 4 + r;
        ox[(size_t)(seg * SEG + qrow) * HD + h * DH + nt * 16 + (lane & 15)] = f2bf(o[i][r]);
      }
  }
}

constexpr float FXS = 4294967296.f;
__global__ __launch_bounds__(256) void k_chain(const u16* __restrict__ mem0, const u16* __restrict__ Wq,
                                               const u16* __restrict__ Wk, const u16* __restrict__ Wv,
                                               const u16* __restrict__ Wo, const u16* __restrict__ qkv,
                                               u16* __restrict__ kvm, u64* gA, u64* accB) {
  __shared__ __attribute__((aligned(16))) u16 sMem[16][584];
  __shared__ __attribute__((aligned(16))) u16 sQ[16][136];
  __shared__ __attribute__((aligned(16))) u16 sK[48][136];
  __shared__ __attribute__((aligned(16))) u16 sV[64][136];
  __shared__ __attribute__((aligned(16))) u16 sP[16][72];
  __shared__ __attribute__((aligned(16))) u16 sO[16][136];
  const int tid = threadIdx.x, lane = tid & 63, wid = tid >> 6;
  const int w = blockIdx.x, h = w >> 3, j = w & 7;
  bf16x8 wf[18];
  if (wid < 3) {
    const u16* W = wid == 0 ? Wq : (wid == 1 ? Wk : Wv);
#pragma unroll
    for (int ks = 0; ks < 18; ++ks) {
#pragma unroll
      for (int e = 0; e < 8; ++e)
        wf[ks][e] = (short)W[(size_t)(ks * 32 + (lane >> 4) * 8 + e) * HD + h * DH + j * 16 + (lane & 15)];
    }
  }
  bf16x8 wo[2][4];
#pragma unroll
  for (int u = 0; u < 2; ++u) {
    int t = (u == 0) ? j + 8 * wid : j + 32;
    bool ok = (u == 0) || (wid == 0 && j < 4);
#pragma unroll
    for (int ks = 0; ks < 4; ++ks)
#pragma unroll
      for (int e = 0; e < 8; ++e)
        wo[u][ks][e] = ok ? (short)Wo[(size_t)(h * DH + ks * 32 + (lane >> 4) * 8 + e) * DM + t * 16 + (lane & 15)] : (short)0;
  }
  for (int i = tid; i < 16 * 584; i += 256) (&sMem[0][0])[i] = 0;
  for (int i = tid; i < 16 * 136; i += 256) { (&sQ[0][0])[i] = 0; (&sO[0][0])[i] = 0; }
  for (int i = tid; i < 64 * 136; i += 256) (&sV[0][0])[i] = 0;
  for (int i = tid; i < 48 * 136; i += 256) (&sK[0][0])[i] = 0;
  for (int i = tid; i < 16 * 72; i += 256) (&sP[0][0])[i] = 0;
  __syncthreads();
  for (int i = tid; i < MEM * DM; i += 256) sMem[i / DM][i % DM] = mem0[i];
  __syncthreads();
  for (int s = 0; s < NSEG; ++s) {
    const unsigned tag = s + 1;
    for (int i = tid; i < 32 * 16 * 2; i += 256) {
      int m = i >> 9, key = (i >> 4) & 31, c = (i & 15) * 8;
      bf16x8 v = *(const bf16x8*)(qkv + (size_t)(s * SEG + key) * QKV_LD + (1 + m) * HD + h * DH + c);
      if (m == 0) *(bf16x8*)&sK[8 + key][c] = v; else *(bf16x8*)&sV[8 + key][c] = v;
    }
    if (s > 0) {
      const u64* src = accB + (size_t)(s - 1) * (MEM * DM);
      for (int i = tid; i < MEM * DM; i += 256) {
        u64 x;
        do { x = __hip_atomic_load((GAS u64*)(src + i), RLX_AGENT); } while (((x + (1ull << 55)) >> 56) != 32ull);
        long long v = (long long)(x - (32ull << 56));
        sMem[i / DM][i % DM] = f2bf((float)v * (1.f / FXS));
      }
    }
    __syncthreads();
    if (wid < 3) {
      f32x4 acc = {0, 0, 0, 0};
#pragma unroll
      for (int ks = 0; ks < 18; ++ks) {
        bf16x8 a = *(const bf16x8*)&sMem[lane & 15][ks * 32 + (lane >> 4) * 8];
        acc = __builtin_amdgcn_mfma_f32_16x16x32_bf16(a, wf[ks], acc, 0, 0, 0);
      }
      if (lane < 32) {
        u64* g = gA + (((size_t)h * 8 + j) * 3 + wid) * 64;
        __hip_atomic_store((GAS u64*)(g + lane), ((u64)tag << 32) | pack2(acc[0], acc[1]), RLX_AGENT);
        __hip_atomic_store((GAS u64*)(g + 32 + lane), ((u64)tag << 32) | pack2(acc[2], acc[3]), RLX_AGENT);
        if (wid > 0) {
          u16* d = kvm + ((size_t)s * 2 + (wid - 1)) * MEM * HD + h * DH + j * 16 + (lane & 15);
#pragma unroll
          for (int r = 0; r < 4; ++r) d[(size_t)((lane >> 4) * 4 + r) * HD] = f2bf(acc[r]);
        }
      }
    }
    for (int i = tid; i < 8 * 3 * 64; i += 256) {
      int jj = i / 192, mat = (i / 64) % 3, g = i & 63;
      const u64* src = gA + (size_t)h * 8 * 3 * 64 + i;
      u64 x;
      do { x = __hip_atomic_load((GAS u64*)src, RLX_AGENT); } while ((unsigned)(x >> 32) != tag);
      int l = g & 31, half = g >> 5;
      int col = jj * 16 + (l & 15), row = (l >> 4) * 4 + half * 2;
      u16 lo = (u16)x, hi = (u16)(x >> 16);
      if (mat == 0) { sQ[row][col] = lo; sQ[row + 1][col] = hi; }
      else if (mat == 1) { sK[row][col] = lo; sK[row + 1][col] = hi; }
      else { sV[row][col] = lo; sV[row + 1][col] = hi; }
    }
    __syncthreads();
    if (wid == 0) {
      f32x4 sc[3] = {f32x4{0, 0, 0, 0}, f32x4{0, 0, 0, 0}, f32x4{0, 0, 0, 0}};
#pragma unroll
      for (int ks = 0; ks < 4; ++ks) {
        bf16x8 a = *(const bf16x8*)&sQ[lane & 15][ks * 32 + (lane >> 4) * 8];
#pragma unroll
        for (int jt = 0; jt < 3; ++jt) {
          bf16x8 b = *(const bf16x8*)&sK[jt * 16 + (lane & 15)][ks * 32 + (lane >> 4) * 8];
          sc[jt] = __builtin_amdgcn_mfma_f32_16x16x32_bf16(a, b, sc[jt], 0, 0, 0);
        }
      }
      const float scale = 0.08838834764831845f;
#pragma unroll
      for (int r = 0; r < 4; ++r) {
        int row = (lane >> 4) * 4 + r;
        float v[3], mx = -3e38f;
#pragma unroll
        for (int jt = 0; jt < 3; ++jt) {
          int key = jt * 16 + (lane & 15);
          v[jt] = key < 40 ? sc[jt][r] * scale : -3e38f;
          mx = fmaxf(mx, v[jt]);
        }
#pragma unroll
        for (int o = 1; o < 16; o <<= 1) mx = fmaxf(mx, __shfl_xor(mx, o));
        float sum = 0.f;
#pragma unroll
        for (int jt = 0; jt < 3; ++jt) { v[jt] = v[jt] > -1e38f ? __expf(v[jt] - mx) : 0.f; sum += v[jt]; }
#pragma unroll
        for (int o = 1; o < 16; o <<= 1) sum += __shfl_xor(sum, o);
        float inv = 1.f / sum;
        if (row < 8) {
#pragma unroll
          for (int jt = 0; jt < 3; ++jt) sP[row][jt * 16 + (lane & 15)] = f2bf(v[jt] * inv);
        }
      }
    }
    __syncthreads();
#pragma unroll
    for (int u = 0; u < 2; ++u) {
      int nt = wid * 2 + u;
      f32x4 o = {0, 0, 0, 0};
#pragma unroll
      for (int ks = 0; ks < 2; ++ks) {
        bf16x8 b;
#pragma unroll
        for (int e = 0; e < 8; ++e) b[e] = (short)sV[ks * 32 + (lane >> 4) * 8 + e][nt * 16 + (lane & 15)];
        bf16x8 a = *(const bf16x8*)&sP[lane & 15][ks * 32 + (lane >> 4) * 8];
        o = __builtin_amdgcn_mfma_f32_16x16x32_bf16(a, b, o, 0, 0, 0);
      }
      if (lane < 32) {
#pragma unroll
        for (int r = 0; r < 4; ++r) sO[(lane >> 4) * 4 + r][nt * 16 + (lane & 15)] = f2bf(o[r]);
      }
    }
    __syncthreads();
    if (s + 1 < NSEG) {
#pragma unroll
      for (int u = 0; u < 2; ++u) {
        if (u == 1 && !(wid == 0 && j < 4)) break;
        int t = (u == 0) ? j + 8 * wid : j + 32;
        f32x4 acc = {0, 0, 0, 0};
#pragma unroll
        for (int ks = 0; ks < 4; ++ks) {
          bf16x8 a = *(const bf16x8*)&sO[lane & 15][ks * 32 + (lane >> 4) * 8];
          acc = __builtin_amdgcn_mfma_f32_16x16x32_bf16(a, wo[u][ks], acc, 0, 0, 0);
        }
        if (lane < 32) {
          u64* dst = accB + (size_t)s * (MEM * DM) + t * 16 + (lane & 15);
#pragma unroll
          for (int r = 0; r < 4; ++r) {
            long long fx = (long long)(acc[r] * FXS);
            __hip_atomic_fetch_add((GAS u64*)(dst + (size_t)((lane >> 4) * 4 + r) * DM), (1ull << 56) + (u64)fx, RLX_AGENT);
          }
        }
      }
    }
    __syncthreads();
  }
}

extern "C" void kernel_launch(void* const* d_in, const int* in_sizes, int n_in, void* d_out, int out_size, void* d_ws,
                              size_t ws_size, hipStream_t stream) {
  const u16* x = (const u16*)d_in[0];
  const u16* mem0 = (const u16*)d_in[1];
  const u16* Wq = (const u16*)d_in[2];
  const u16* Wk = (const u16*)d_in[3];
  const u16* Wv = (const u16*)d_in[4];
  const u16* Wo = (const u16*)d_in[5];
  const u16* Wg = (const u16*)d_in[8];
  const u16* Wu = (const u16*)d_in[9];
  const u16* Wd = (const u16*)d_in[10];
  const u16* c = (const u16*)d_in[11];
  const u16* wmod = (const u16*)d_in[12];
  char* ws = (char*)d_ws;
  u64* accB = (u64*)ws;
  u64* gA = (u64*)(ws + 2359296);
  size_t zbytes = 2359296 + 393216;
  char* p = ws + zbytes;
  float* mod = (float*)p; p += 6 * DM * 4 + 256;
  u16* h1 = (u16*)p; p += (size_t)S_ * DM * 2;
  u16* qkv = (u16*)p; p += (size_t)S_ * QKV_LD * 2;
  u16* kvm = (u16*)p; p += (size_t)NSEG * 2 * MEM * HD * 2;
  u16* ox = (u16*)p; p += (size_t)S_ * HD * 2;
  float* x1 = (float*)p; p += (size_t)S_ * DM * 4;
  u16* h2 = (u16*)p; p += (size_t)S_ * DM * 2;
  u16* hff = (u16*)p; p += (size_t)S_ * FF * 2;
  hipMemsetAsync(ws, 0, zbytes, stream);
  k_mod<<<6 * DM / 64, 256, 0, stream>>>(c, wmod, mod);
  k_ln<false><<<S_ / 4, 256, 0, stream>>>(x, mod + 0 * DM, mod + 1 * DM, h1);
  k_gemm<128, 0><<<dim3(HD / 128, S_ / 128), 256, 0, stream>>>(h1, Wq, nullptr, DM, HD, QKV_LD, qkv, nullptr, nullptr);
  k_gemm<128, 0><<<dim3(HD / 128, S_ / 128), 256, 0, stream>>>(h1, Wk, nullptr, DM, HD, QKV_LD, qkv + HD, nullptr, nullptr);
  k_gemm<128, 0><<<dim3(HD / 128, S_ / 128), 256, 0, stream>>>(h1, Wv, nullptr, DM, HD, QKV_LD, qkv + 2 * HD, nullptr, nullptr);
  k_chain<<<256, 256, 0, stream>>>(mem0, Wq, Wk, Wv, Wo, qkv, kvm, gA, accB);
  k_xattn<<<NSEG * NH / 4, 256, 0, stream>>>(qkv, kvm, ox);
  k_gemm<64, 1><<<dim3(DM / 64, S_ / 128), 256, 0, stream>>>(ox, Wo, nullptr, HD, DM, DM, x1, x, mod + 2 * DM);
  k_ln<true><<<S_ / 4, 256, 0, stream>>>(x1, mod + 3 * DM, mod + 4 * DM, h2);
  k_gemm<64, 2><<<dim3(FF / 64, S_ / 128), 256, 0, stream>>>(h2, Wg, Wu, DM, FF, FF, hff, nullptr, nullptr);
  k_gemm<64, 3><<<dim3(DM / 64, S_ / 128), 256, 0, stream>>>(hff, Wd, nullptr, FF, DM, DM, d_out, x1, mod + 5 * DM);
}
```

```cpp
#include <hip/hip_runtime.h>
#include <hip/hip_bf16.h>
#include <stdint.h>

typedef __attribute__((ext_vector_type(8))) short bf16x8;
typedef __attribute__((ext_vector_type(4))) short s16x4;
typedef __attribute__((ext_vector_type(4))) float f32x4;
typedef unsigned short u16;
typedef unsigned long long u64;

#define GAS __attribute__((address_space(1)))
#define LAS __attribute__((address_space(3)))
#define RLX_AGENT __ATOMIC_RELAXED, __HIP_MEMORY_SCOPE_AGENT

constexpr int S_ = 2048, DM = 576, NH = 32, DH = 128, HD = NH * DH;
constexpr int MEM = 8, SEG = 32, NSEG = 64, FF = 4608;
constexpr int QKV_LD = 3 * HD;

__device__ __forceinline__ float bf2f(u16 v) { return __uint_as_float(((unsigned)v) << 16); }
__device__ __forceinline__ u16 f2bf(float f) {
  __hip_bfloat16 b = __float2bfloat16(f);
  return *reinterpret_cast<u16*>(&b);
}
__device__ __forceinline__ unsigned pack2(float a, float b) { return (unsigned)f2bf(a) | ((unsigned)f2bf(b) << 16); }

__global__ void k_mod(const u16* __restrict__ c, const u16* __restrict__ wmod, float* __restrict__ mod) {
  __shared__ float red[16][17];
  int cl = threadIdx.x & 15, ks = threadIdx.x >> 4;
  int col = blockIdx.x * 16 + cl;
  float acc = 0.f;
#pragma unroll 12
  for (int k = ks * 36; k < ks * 36 + 36; ++k) {
    float cv = bf2f(c[k]);
    float sv = cv / (1.f + __expf(-cv));
    acc += sv * bf2f(wmod[(size_t)k * (6 * DM) + col]);
  }
  red[ks][cl] = acc;
  __syncthreads();
  if (ks == 0) {
    float t = 0.f;
#pragma unroll
    for (int i = 0; i < 16; ++i) t += red[i][cl];
    mod[col] = t;
  }
}

template <bool XF32>
__global__ void k_ln(const void* __restrict__ xin, const float* __restrict__ shift, const float* __restrict__ scale,
                     u16* __restrict__ h) {
  int row = blockIdx.x * 4 + (threadIdx.x >> 6);
  int lane = threadIdx.x & 63;
  float v[9];
  float s = 0.f;
#pragma unroll
  for (int i = 0; i < 9; ++i) {
    int d = lane + i * 64;
    if (XF32) v[i] = ((const float*)xin)[(size_t)row * DM + d];
    else v[i] = bf2f(((const u16*)xin)[(size_t)row * DM + d]);
    s += v[i];
  }
#pragma unroll
  for (int o = 32; o > 0; o >>= 1) s += __shfl_xor(s, o);
  float m = s * (1.f / DM);
  float q = 0.f;
#pragma unroll
  for (int i = 0; i < 9; ++i) { float d = v[i] - m; q += d * d; }
#pragma unroll
  for (int o = 32; o > 0; o >>= 1) q += __shfl_xor(q, o);
  float r = rsqrtf(q * (1.f / DM) + 1e-5f);
#pragma unroll
  for (int i = 0; i < 9; ++i) {
    int d = lane + i * 64;
    h[(size_t)row * DM + d] = f2bf((v[i] - m) * r * (1.f + scale[d]) + shift[d]);
  }
}

__device__ __forceinline__ s16x4 tr_read(const u16* p) {
  typedef __attribute__((ext_vector_type(4))) short v4i16_t;
  return __builtin_bit_cast(s16x4, __builtin_amdgcn_ds_read_tr16_b64_v4i16((LAS v4i16_t*)p));
}

template <int BN, int EPI>
__global__ __launch_bounds__(256, 2) void k_gemm(const u16* __restrict__ A, const u16* __restrict__ B,
                                                 const u16* __restrict__ B2, int K, int ldb, int N,
                                                 void* __restrict__ Cout, const void* __restrict__ resid,
                                                 const float* __restrict__ gvec) {
  constexpr int BM = 128, BK = 64;
  constexpr int NB = (EPI == 2) ? 2 : 1;
  constexpr int A_BYTES = BM * BK * 2;
  constexpr int B_BYTES = BK * BN * 2;
  constexpr int STAGE = A_BYTES + NB * B_BYTES;
  __shared__ __attribute__((aligned(16))) char lds[2 * STAGE];
  const int tid = threadIdx.x, lane = tid & 63, wid = tid >> 6;
  const int wr = wid >> 1, wc = wid & 1;
  const int bm = blockIdx.y * BM, bn = blockIdx.x * BN;
  constexpr int WN = BN / 2;
  constexpr int NT = WN / 16;
  f32x4 acc[NB][4][NT];
#pragma unroll
  for (int b = 0; b < NB; ++b)
#pragma unroll
    for (int i = 0; i < 4; ++i)
#pragma unroll
      for (int j = 0; j < NT; ++j) acc[b][i][j] = f32x4{0.f, 0.f, 0.f, 0.f};

  auto stage = [&](int buf, int kt) {
    char* base = lds + buf * STAGE;
#pragma unroll
    for (int i = 0; i < 4; ++i) {
      int c = tid + i * 256;
      int row = c >> 3, pos = c & 7, ch = pos ^ (row & 7);
      const u16* src = A + (size_t)(bm + row) * K + kt * BK + ch * 8;
      __builtin_amdgcn_global_load_lds((const GAS void*)src, (LAS void*)(base + (i * 256 + wid * 64) * 16), 16, 0, 0);
    }
    constexpr int CPR = BN / 8;
    constexpr int NCH = BK * CPR;
#pragma unroll
    for (int b = 0; b < NB; ++b) {
      const u16* Bm = b ? B2 : B;
#pragma unroll
      for (int i = 0; i < NCH / 256; ++i) {
        int c = tid + i * 256;
        int row = c / CPR, pos = c % CPR;
        int ch = pos ^ ((((row & 3) << 2) | ((row >> 2) & 3)) & (CPR - 1));
        const u16* src = Bm + (size_t)(kt * BK + row) * ldb + bn + ch * 8;
        __builtin_amdgcn_global_load_lds((const GAS void*)src,
                                         (LAS void*)(base + A_BYTES + b * B_BYTES + (i * 256 + wid * 64) * 16), 16, 0, 0);
      }
    }
  };

  const int nkt = K / BK;
  stage(0, 0);
  asm volatile("s_waitcnt vmcnt(0)" ::: "memory");
  __syncthreads();
  for (int kt = 0; kt < nkt; ++kt) {
    int cur = kt & 1;
    if (kt + 1 < nkt) stage(cur ^ 1, kt + 1);
    const char* base = lds + cur * STAGE;
#pragma unroll
    for (int kk = 0; kk < 2; ++kk) {
      bf16x8 af[4];
#pragma unroll
      for (int i = 0; i < 4; ++i) {
        int row = wr * 64 + i * 16 + (lane & 15);
        int ch = (kk * 4 + (lane >> 4)) ^ (row & 7);
        af[i] = *(const bf16x8*)(base + row * 128 + ch * 16);
      }
#pragma unroll
      for (int b = 0; b < NB; ++b) {
#pragma unroll
        for (int j = 0; j < NT; ++j) {
          bf16x8 bfr;
#pragma unroll
          for (int t = 0; t < 2; ++t) {
            int krow = kk * 32 + 8 * (lane >> 4) + 4 * t + ((lane & 15) >> 2);
            int col = wc * WN + j * 16 + 4 * (lane & 3);
            int ch = (col >> 3) ^ ((((krow & 3) << 2) | ((krow >> 2) & 3)) & (BN / 8 - 1));
            const u16* p = (const u16*)(base + A_BYTES + b * B_BYTES + krow * (BN * 2) + ch * 16 + (col & 7) * 2);
            s16x4 v = tr_read(p);
            bfr[4 * t + 0] = v[0]; bfr[4 * t + 1] = v[1]; bfr[4 * t + 2] = v[2]; bfr[4 * t + 3] = v[3];
          }
#pragma unroll
          for (int i = 0; i < 4; ++i)
            acc[b][i][j] = __builtin_amdgcn_mfma_f32_16x16x32_bf16(af[i], bfr, acc[b][i][j], 0, 0, 0);
        }
      }
    }
    asm volatile("s_waitcnt vmcnt(0)" ::: "memory");
    __syncthreads();
  }
#pragma unroll
  for (int i = 0; i < 4; ++i)
#pragma unroll
    for (int j = 0; j < NT; ++j)
#pragma unroll
      for (int r = 0; r < 4; ++r) {
        int row = bm + wr * 64 + i * 16 + (lane >> 4) * 4 + r;
        int col = bn + wc * WN + j * 16 + (lane & 15);
        float v = acc[0][i][j][r];
        if (EPI == 0) {
          ((u16*)Cout)[(size_t)row * N + col] = f2bf(v);
        } else if (EPI == 1) {
          float xv = bf2f(((const u16*)resid)[(size_t)row * DM + col]);
          ((float*)Cout)[(size_t)row * DM + col] = xv + gvec[col] * v;
        } else if (EPI == 2) {
          float g = bf2f(f2bf(v)), u = bf2f(f2bf(acc[NB - 1][i][j][r]));
          g = g > 0.f ? g : 0.f;
          ((u16*)Cout)[(size_t)row * N + col] = f2bf(g * u);
        } else {
          float xv = ((const float*)resid)[(size_t)row * DM + col];
          ((u16*)Cout)[(size_t)row * DM + col] = f2bf(xv + gvec[col] * v);
        }
      }
}


__global__ __launch_bounds__(256) void k_xattn(const u16* __restrict__ qkv, const u16* __restrict__ kvm,
                                               u16* __restrict__ ox) {
  __shared__ __attribute__((aligned(16))) u16 sP[4][32][72];
  __shared__ __attribute__((aligned(16))) u16 sV[4][64][136];
  const int lane = threadIdx.x & 63, wid = threadIdx.x >> 6;
  const int pair = blockIdx.x * 4 + wid;
  const int seg = pair >> 5, h = pair & 31;
  const u16* qbase = qkv + (size_t)(seg * SEG) * QKV_LD + h * DH;
  const u16* kbase = qbase + HD;
  const u16* vbase = qbase + 2 * HD;
  const u16* kmb = kvm + ((size_t)seg * 2 + 0) * MEM * HD + h * DH;
  const u16* vmb = kvm + ((size_t)seg * 2 + 1) * MEM * HD + h * DH;
  for (int i = lane; i < 64 * 16; i += 64) {
    int key = i >> 4, c = (i & 15) * 8;
    bf16x8 v = {0, 0, 0, 0, 0, 0, 0, 0};
    if (key < 8) v = *(const bf16x8*)(vmb + (size_t)key * HD + c);
    else if (key < 40) v = *(const bf16x8*)(vbase + (size_t)(key - 8) * QKV_LD + c);
    *(bf16x8*)&sV[wid][key][c] = v;
  }
  f32x4 sc[2][3];
#pragma unroll
  for (int i = 0; i < 2; ++i)
#pragma unroll
    for (int j = 0; j < 3; ++j) sc[i][j] = f32x4{0, 0, 0, 0};
#pragma unroll
  for (int ks = 0; ks < 4; ++ks) {
    int kd = ks * 32 + (lane >> 4) * 8;
    bf16x8 a[2], b[3];
#pragma unroll
    for (int i = 0; i < 2; ++i) a[i] = *(const bf16x8*)(qbase + (size_t)(i * 16 + (lane & 15)) * QKV_LD + kd);
#pragma unroll
    for (int j = 0; j < 3; ++j) {
      int key = j * 16 + (lane & 15);
      if (key < 8) b[j] = *(const bf16x8*)(kmb + (size_t)key * HD + kd);
      else if (key < 40) b[j] = *(const bf16x8*)(kbase + (size_t)(key - 8) * QKV_LD + kd);
      else b[j] = bf16x8{0, 0, 0, 0, 0, 0, 0, 0};
    }
#pragma unroll
    for (int i = 0; i < 2; ++i)
#pragma unroll
      for (int j = 0; j < 3; ++j) sc[i][j] = __builtin_amdgcn_mfma_f32_16x16x32_bf16(a[i], b[j], sc[i][j], 0, 0, 0);
  }
  const float scale = 0.08838834764831845f;
#pragma unroll
  for (int i = 0; i < 2; ++i)
#pragma unroll
    for (int r = 0; r < 4; ++r) {
      int qrow = i * 16 + (lane >> 4) * 4 + r;
      float v[3];
      float mx = -3e38f;
#pragma unroll
      for (int j = 0; j < 3; ++j) {
        int key = j * 16 + (lane & 15);
        bool ok = (key < 8) || (key < 40 && (key - 8) <= qrow);
        v[j] = ok ? sc[i][j][r] * scale : -3e38f;
        mx = fmaxf(mx, v[j]);
      }
#pragma unroll
      for (int o = 1; o < 16; o <<= 1) mx = fmaxf(mx, __shfl_xor(mx, o));
      float sum = 0.f;
#pragma unroll
      for (int j = 0; j < 3; ++j) { v[j] = v[j] > -1e38f ? __expf(v[j] - mx) : 0.f; sum += v[j]; }
#pragma unroll
      for (int o = 1; o < 16; o <<= 1) sum += __shfl_xor(sum, o);
      float inv = 1.f / sum;
#pragma unroll
      for (int j = 0; j < 3; ++j) sP[wid][qrow][j * 16 + (lane & 15)] = f2bf(v[j] * inv);
      sP[wid][qrow][48 + (lane & 15)] = 0;
    }
  __syncthreads();
#pragma unroll
  for (int nt = 0; nt < 8; ++nt) {
    f32x4 o[2] = {f32x4{0, 0, 0, 0}, f32x4{0, 0, 0, 0}};
#pragma unroll
    for (int ks = 0; ks < 2; ++ks) {
      bf16x8 b;
#pragma unroll
      for (int t = 0; t < 2; ++t) {
        s16x4 v = tr_read(&sV[wid][ks * 32 + (lane >> 4) * 8 + 4 * t + ((lane & 15) >> 2)][nt * 16 + 4 * (lane & 3)]);
        b[4 * t + 0] = v[0]; b[4 * t + 1] = v[1]; b[4 * t + 2] = v[2]; b[4 * t + 3] = v[3];
      }
#pragma unroll
      for (int i = 0; i < 2; ++i) {
        bf16x8 a = *(const bf16x8*)&sP[wid][i * 16 + (lane & 15)][ks * 32 + (lane >> 4) * 8];
        o[i] = __builtin_amdgcn_mfma_f32_16x16x32_bf16(a, b, o[i], 0, 0, 0);
      }
    }
#pragma unroll
    for (int i = 0; i < 2; ++i)
#pragma unroll
      for (int r = 0; r < 4; ++r) {
        int qrow = i * 16 + (lane >> 4) * 4 + r;
        ox[(size_t)(seg * SEG + qrow) * HD + h * DH + nt * 16 + (lane & 15)] = f2bf(o[i][r]);
      }
  }
}

constexpr float FXS = 4294967296.f;
__global__ __launch_bounds__(256) void k_chain(const u16* __restrict__ mem0, const u16* __restrict__ Wq,
                                               const u16* __restrict__ Wk, const u16* __restrict__ Wv,
                                               const u16* __restrict__ Wo, const u16* __restrict__ qkv,
                                               u16* __restrict__ kvm, u64* gA, u64* accB) {
  __shared__ __attribute__((aligned(16))) u16 sMem[16][584];
  __shared__ __attribute__((aligned(16))) u16 sQ[16][136];
  __shared__ __attribute__((aligned(16))) u16 sK[48][136];
  __shared__ __attribute__((aligned(16))) u16 sV[64][136];
  __shared__ __attribute__((aligned(16))) u16 sP[16][72];
  __shared__ __attribute__((aligned(16))) u16 sO[16][136];
  const int tid = threadIdx.x, lane = tid & 63, wid = tid >> 6;
  const int w = blockIdx.x, h = w >> 3, j = w & 7;
  bf16x8 wf[18];
  if (wid < 3) {
    const u16* W = wid == 0 ? Wq : (wid == 1 ? Wk : Wv);
#pragma unroll
    for (int ks = 0; ks < 18; ++ks) {
#pragma unroll
      for (int e = 0; e < 8; ++e)
        wf[ks][e] = (short)W[(size_t)(ks * 32 + (lane >> 4) * 8 + e) * HD + h * DH + j * 16 + (lane & 15)];
    }
  }
  bf16x8 wo[2][4];
#pragma unroll
  for (int u = 0; u < 2; ++u) {
    int t = (u == 0) ? j + 8 * wid : j + 32;
    bool ok = (u == 0) || (wid == 0 && j < 4);
#pragma unroll
    for (int ks = 0; ks < 4; ++ks)
#pragma unroll
      for (int e = 0; e < 8; ++e)
        wo[u][ks][e] = ok ? (short)Wo[(size_t)(h * DH + ks * 32 + (lane >> 4) * 8 + e) * DM + t * 16 + (lane & 15)] : (short)0;
  }
  for (int i = tid; i < 16 * 584; i += 256) (&sMem[0][0])[i] = 0;
  for (int i = tid; i < 16 * 136; i += 256) { (&sQ[0][0])[i] = 0; (&sO[0][0])[i] = 0; }
  for (int i = tid; i < 64 * 136; i += 256) (&sV[0][0])[i] = 0;
  for (int i = tid; i < 48 * 136; i += 256) (&sK[0][0])[i] = 0;
  for (int i = tid; i < 16 * 72; i += 256) (&sP[0][0])[i] = 0;
  __syncthreads();
  for (int i = tid; i < MEM * DM; i += 256) sMem[i / DM][i % DM] = mem0[i];
  __syncthreads();
  for (int s = 0; s < NSEG; ++s) {
    const unsigned tag = s + 1;
    for (int i = tid; i < 32 * 16 * 2; i += 256) {
      int m = i >> 9, key = (i >> 4) & 31, c = (i & 15) * 8;
      bf16x8 v = *(const bf16x8*)(qkv + (size_t)(s * SEG + key) * QKV_LD + (1 + m) * HD + h * DH + c);
      if (m == 0) *(bf16x8*)&sK[8 + key][c] = v; else *(bf16x8*)&sV[8 + key][c] = v;
    }
    if (s > 0) {
      const u64* src = accB + (size_t)(s - 1) * (MEM * DM);
      u64 xs[18];
      for (;;) {
        bool ok = true;
#pragma unroll
        for (int i = 0; i < 18; ++i) xs[i] = __hip_atomic_load((GAS u64*)(src + tid + i * 256), RLX_AGENT);
#pragma unroll
        for (int i = 0; i < 18; ++i) ok &= (((xs[i] + (1ull << 55)) >> 56) == 32ull);
        if (ok) break;
        __builtin_amdgcn_s_sleep(1);
      }
#pragma unroll
      for (int i = 0; i < 18; ++i) {
        int e = tid + i * 256;
        long long v = (long long)(xs[i] - (32ull << 56));
        sMem[e / DM][e % DM] = f2bf((float)v * (1.f / FXS));
      }
    }
    __syncthreads();
    if (wid < 3) {
      f32x4 acc = {0, 0, 0, 0};
#pragma unroll
      for (int ks = 0; ks < 18; ++ks) {
        bf16x8 a = *(const bf16x8*)&sMem[lane & 15][ks * 32 + (lane >> 4) * 8];
        acc = __builtin_amdgcn_mfma_f32_16x16x32_bf16(a, wf[ks], acc, 0, 0, 0);
      }
      if (lane < 32) {
        u64* g = gA + (((size_t)h * 8 + j) * 3 + wid) * 64;
        __hip_atomic_store((GAS u64*)(g + lane), ((u64)tag << 32) | pack2(acc[0], acc[1]), RLX_AGENT);
        __hip_atomic_store((GAS u64*)(g + 32 + lane), ((u64)tag << 32) | pack2(acc[2], acc[3]), RLX_AGENT);
        if (wid > 0) {
          u16* d = kvm + ((size_t)s * 2 + (wid - 1)) * MEM * HD + h * DH + j * 16 + (lane & 15);
#pragma unroll
          for (int r = 0; r < 4; ++r) d[(size_t)((lane >> 4) * 4 + r) * HD] = f2bf(acc[r]);
        }
      }
    }
    {
      u64 xs[6];
      const u64* src = gA + (size_t)h * 8 * 3 * 64 + tid;
      for (;;) {
        bool ok = true;
#pragma unroll
        for (int i = 0; i < 6; ++i) xs[i] = __hip_atomic_load((GAS u64*)(src + i * 256), RLX_AGENT);
#pragma unroll
        for (int i = 0; i < 6; ++i) ok &= ((unsigned)(xs[i] >> 32) == tag);
        if (ok) break;
        __builtin_amdgcn_s_sleep(1);
      }
#pragma unroll
      for (int ii = 0; ii < 6; ++ii) {
        int i = tid + ii * 256;
        int jj = i / 192, mat = (i / 64) % 3, g = i & 63;
        u64 x = xs[ii];
        int l = g & 31, half = g >> 5;
        int col = jj * 16 + (l & 15), row = (l >> 4) * 4 + half * 2;
        u16 lo = (u16)x, hi = (u16)(x >> 16);
        if (mat == 0) { sQ[row][col] = lo; sQ[row + 1][col] = hi; }
        else if (mat == 1) { sK[row][col] = lo; sK[row + 1][col] = hi; }
        else { sV[row][col] = lo; sV[row + 1][col] = hi; }
      }
    }
    __syncthreads();
    if (wid == 0) {
      f32x4 sc[3] = {f32x4{0, 0, 0, 0}, f32x4{0, 0, 0, 0}, f32x4{0, 0, 0, 0}};
#pragma unroll
      for (int ks = 0; ks < 4; ++ks) {
        bf16x8 a = *(const bf16x8*)&sQ[lane & 15][ks * 32 + (lane >> 4) * 8];
#pragma unroll
        for (int jt = 0; jt < 3; ++jt) {
          bf16x8 b = *(const bf16x8*)&sK[jt * 16 + (lane & 15)][ks * 32 + (lane >> 4) * 8];
          sc[jt] = __builtin_amdgcn_mfma_f32_16x16x32_bf16(b, a, sc[jt], 0, 0, 0);
        }
      }
      const float scale = 0.08838834764831845f;
      float v[3][4], mx = -3e38f;
#pragma unroll
      for (int jt = 0; jt < 3; ++jt)
#pragma unroll
        for (int r = 0; r < 4; ++r) {
          int key = jt * 16 + (lane >> 4) * 4 + r;
          v[jt][r] = key < 40 ? sc[jt][r] * scale : -3e38f;
          mx = fmaxf(mx, v[jt][r]);
        }
      mx = fmaxf(mx, __shfl_xor(mx, 16));
      mx = fmaxf(mx, __shfl_xor(mx, 32));
      float sum = 0.f;
#pragma unroll
      for (int jt = 0; jt < 3; ++jt)
#pragma unroll
        for (int r = 0; r < 4; ++r) { v[jt][r] = v[jt][r] > -1e38f ? __expf(v[jt][r] - mx) : 0.f; sum += v[jt][r]; }
      sum += __shfl_xor(sum, 16);
      sum += __shfl_xor(sum, 32);
      float inv = 1.f / sum;
      if ((lane & 15) < 8) {
#pragma unroll
        for (int jt = 0; jt < 3; ++jt) {
          unsigned lo = pack2(v[jt][0] * inv, v[jt][1] * inv), hi = pack2(v[jt][2] * inv, v[jt][3] * inv);
          *(uint2*)&sP[lane & 15][jt * 16 + (lane >> 4) * 4] = make_uint2(lo, hi);
        }
      }
    }
    __syncthreads();
#pragma unroll
    for (int u = 0; u < 2; ++u) {
      int nt = wid * 2 + u;
      f32x4 o = {0, 0, 0, 0};
#pragma unroll
      for (int ks = 0; ks < 2; ++ks) {
        bf16x8 b;
#pragma unroll
        for (int t = 0; t < 2; ++t) {
          s16x4 v = tr_read(&sV[ks * 32 + (lane >> 4) * 8 + 4 * t + ((lane & 15) >> 2)][nt * 16 + 4 * (lane & 3)]);
          b[4 * t + 0] = v[0]; b[4 * t + 1] = v[1]; b[4 * t + 2] = v[2]; b[4 * t + 3] = v[3];
        }
        bf16x8 a = *(const bf16x8*)&sP[lane & 15][ks * 32 + (lane >> 4) * 8];
        o = __builtin_amdgcn_mfma_f32_16x16x32_bf16(a, b, o, 0, 0, 0);
      }
      if (lane < 32) {
#pragma unroll
        for (int r = 0; r < 4; ++r) sO[(lane >> 4) * 4 + r][nt * 16 + (lane & 15)] = f2bf(o[r]);
      }
    }
    __syncthreads();
    if (s + 1 < NSEG) {
#pragma unroll
      for (int u = 0; u < 2; ++u) {
        if (u == 1 && !(wid == 0 && j < 4)) break;
        int t = (u == 0) ? j + 8 * wid : j + 32;
        f32x4 acc = {0, 0, 0, 0};
#pragma unroll
        for (int ks = 0; ks < 4; ++ks) {
          bf16x8 a = *(const bf16x8*)&sO[lane & 15][ks * 32 + (lane >> 4) * 8];
          acc = __builtin_amdgcn_mfma_f32_16x16x32_bf16(a, wo[u][ks], acc, 0, 0, 0);
        }
        if (lane < 32) {
          u64* dst = accB + (size_t)s * (MEM * DM) + t * 16 + (lane & 15);
#pragma unroll
          for (int r = 0; r < 4; ++r) {
            long long fx = (long long)(acc[r] * FXS);
            __hip_atomic_fetch_add((GAS u64*)(dst + (size_t)((lane >> 4) * 4 + r) * DM), (1ull << 56) + (u64)fx, RLX_AGENT);
          }
        }
      }
    }
    __syncthreads();
  }
}

extern "C" void kernel_launch(void* const* d_in, const int* in_sizes, int n_in, void* d_out, int out_size, void* d_ws,
                              size_t ws_size, hipStream_t stream) {
  const u16* x = (const u16*)d_in[0];
  const u16* mem0 = (const u16*)d_in[1];
  const u16* Wq = (const u16*)d_in[2];
  const u16* Wk = (const u16*)d_in[3];
  const u16* Wv = (const u16*)d_in[4];
  const u16* Wo = (const u16*)d_in[5];
  const u16* Wg = (const u16*)d_in[8];
  const u16* Wu = (const u16*)d_in[9];
  const u16* Wd = (const u16*)d_in[10];
  const u16* c = (const u16*)d_in[11];
  const u16* wmod = (const u16*)d_in[12];
  char* ws = (char*)d_ws;
  u64* accB = (u64*)ws;
  u64* gA = (u64*)(ws + 2359296);
  size_t zbytes = 2359296 + 393216;
  char* p = ws + zbytes;
  float* mod = (float*)p; p += 6 * DM * 4 + 256;
  u16* h1 = (u16*)p; p += (size_t)S_ * DM * 2;
  u16* qkv = (u16*)p; p += (size_t)S_ * QKV_LD * 2;
  u16* kvm = (u16*)p; p += (size_t)NSEG * 2 * MEM * HD * 2;
  u16* ox = (u16*)p; p += (size_t)S_ * HD * 2;
  float* x1 = (float*)p; p += (size_t)S_ * DM * 4;
  u16* h2 = (u16*)p; p += (size_t)S_ * DM * 2;
  u16* hff = (u16*)p; p += (size_t)S_ * FF * 2;
  hipMemsetAsync(ws, 0, zbytes, stream);
  k_mod<<<6 * DM / 16, 256, 0, stream>>>(c, wmod, mod);
  k_ln<false><<<S_ / 4, 256, 0, stream>>>(x, mod + 0 * DM, mod + 1 * DM, h1);
  k_gemm<128, 0><<<dim3(HD / 128, S_ / 128), 256, 0, stream>>>(h1, Wq, nullptr, DM, HD, QKV_LD, qkv, nullptr, nullptr);
  k_gemm<128, 0><<<dim3(HD / 128, S_ / 128), 256, 0, stream>>>(h1, Wk, nullptr, DM, HD, QKV_LD, qkv + HD, nullptr, nullptr);
  k_gemm<128, 0><<<dim3(HD / 128, S_ / 128), 256, 0, stream>>>(h1, Wv, nullptr, DM, HD, QKV_LD, qkv + 2 * HD, nullptr, nullptr);
  k_chain<<<256, 256, 0, stream>>>(mem0, Wq, Wk, Wv, Wo, qkv, kvm, gA, accB);
  k_xattn<<<NSEG * NH / 4, 256, 0, stream>>>(qkv, kvm, ox);
  k_gemm<64, 1><<<dim3(DM / 64, S_ / 128), 256, 0, stream>>>(ox, Wo, nullptr, HD, DM, DM, x1, x, mod + 2 * DM);
  k_ln<true><<<S_ / 4, 256, 0, stream>>>(x1, mod + 3 * DM, mod + 4 * DM, h2);
  k_gemm<64, 2><<<dim3(FF / 64, S_ / 128), 256, 0, stream>>>(h2, Wg, Wu, DM, FF, FF, hff, nullptr, nullptr);
  k_gemm<64, 3><<<dim3(DM / 64, S_ / 128), 256, 0, stream>>>(hff, Wd, nullptr, FF, DM, DM, d_out, x1, mod + 5 * DM);
}
```

```cpp
#include <hip/hip_runtime.h>
#include <hip/hip_bf16.h>
#include <stdint.h>

typedef __attribute__((ext_vector_type(8))) short bf16x8;
typedef __attribute__((ext_vector_type(4))) short s16x4;
typedef __attribute__((ext_vector_type(4))) float f32x4;
typedef unsigned short u16;
typedef unsigned long long u64;

#define GAS __attribute__((address_space(1)))
#define LAS __attribute__((address_space(3)))
#define RLX_AGENT __ATOMIC_RELAXED, __HIP_MEMORY_SCOPE_AGENT

constexpr int S_ = 2048, DM = 576, NH = 32, DH = 128, HD = NH * DH;
constexpr int MEM = 8, SEG = 32, NSEG = 64, FF = 4608;
constexpr int QKV_LD = 3 * HD;

__device__ __forceinline__ float bf2f(u16 v) { return __uint_as_float(((unsigned)v) << 16); }
__device__ __forceinline__ u16 f2bf(float f) {
  __hip_bfloat16 b = __float2bfloat16(f);
  return *reinterpret_cast<u16*>(&b);
}
__device__ __forceinline__ unsigned pack2(float a, float b) { return (unsigned)f2bf(a) | ((unsigned)f2bf(b) << 16); }

__global__ void k_mod(const u16* __restrict__ c, const u16* __restrict__ wmod, float* __restrict__ mod) {
  __shared__ float red[16][64];
  int cl = threadIdx.x & 63, ks = threadIdx.x >> 6;
  int col = blockIdx.x * 64 + cl;
  float acc = 0.f;
#pragma unroll 12
  for (int k = ks * 36; k < ks * 36 + 36; ++k) {
    float cv = bf2f(c[k]);
    float sv = cv / (1.f + __expf(-cv));
    acc += sv * bf2f(wmod[(size_t)k * (6 * DM) + col]);
  }
  red[ks][cl] = acc;
  __syncthreads();
  if (ks == 0) {
    float t = 0.f;
#pragma unroll
    for (int i = 0; i < 16; ++i) t += red[i][cl];
    mod[col] = t;
  }
}

template <bool XF32>
__global__ void k_ln(const void* __restrict__ xin, const float* __restrict__ shift, const float* __restrict__ scale,
                     u16* __restrict__ h) {
  int row = blockIdx.x * 4 + (threadIdx.x >> 6);
  int lane = threadIdx.x & 63;
  float v[9];
  float s = 0.f;
#pragma unroll
  for (int i = 0; i < 9; ++i) {
    int d = lane + i * 64;
    if (XF32) v[i] = ((const float*)xin)[(size_t)row * DM + d];
    else v[i] = bf2f(((const u16*)xin)[(size_t)row * DM + d]);
    s += v[i];
  }
#pragma unroll
  for (int o = 32; o > 0; o >>= 1) s += __shfl_xor(s, o);
  float m = s * (1.f / DM);
  float q = 0.f;
#pragma unroll
  for (int i = 0; i < 9; ++i) { float d = v[i] - m; q += d * d; }
#pragma unroll
  for (int o = 32; o > 0; o >>= 1) q += __shfl_xor(q, o);
  float r = rsqrtf(q * (1.f / DM) + 1e-5f);
#pragma unroll
  for (int i = 0; i < 9; ++i) {
    int d = lane + i * 64;
    h[(size_t)row * DM + d] = f2bf((v[i] - m) * r * (1.f + scale[d]) + shift[d]);
  }
}

__device__ __forceinline__ s16x4 tr_read(const u16* p) {
  typedef __attribute__((ext_vector_type(4))) short v4i16_t;
  return __builtin_bit_cast(s16x4, __builtin_amdgcn_ds_read_tr16_b64_v4i16((LAS v4i16_t*)p));
}

template <int BM, int BN, int EPI>
__global__ __launch_bounds__(256, 2) void k_gemm(const u16* __restrict__ A, const u16* __restrict__ B,
                                                 const u16* __restrict__ B2, int K, int ldb, int N,
                                                 void* __restrict__ Cout, const void* __restrict__ resid,
                                                 const float* __restrict__ gvec) {
  constexpr int BK = 64;
  constexpr int MT = BM / 32, WM = BM / 2;
  constexpr int NB = (EPI == 2) ? 2 : 1;
  constexpr int A_BYTES = BM * BK * 2;
  constexpr int B_BYTES = BK * BN * 2;
  constexpr int STAGE = A_BYTES + NB * B_BYTES;
  __shared__ __attribute__((aligned(16))) char lds[2 * STAGE];
  const int tid = threadIdx.x, lane = tid & 63, wid = tid >> 6;
  const int wr = wid >> 1, wc = wid & 1;
  const int bm = blockIdx.y * BM, bn = blockIdx.x * BN;
  constexpr int WN = BN / 2;
  constexpr int NT = WN / 16;
  f32x4 acc[NB][MT][NT];
#pragma unroll
  for (int b = 0; b < NB; ++b)
#pragma unroll
    for (int i = 0; i < MT; ++i)
#pragma unroll
      for (int j = 0; j < NT; ++j) acc[b][i][j] = f32x4{0.f, 0.f, 0.f, 0.f};

  auto stage = [&](int buf, int kt) {
    char* base = lds + buf * STAGE;
#pragma unroll
    for (int i = 0; i < BM / 32; ++i) {
      int c = tid + i * 256;
      int row = c >> 3, pos = c & 7, ch = pos ^ (row & 7);
      const u16* src = A + (size_t)(bm + row) * K + kt * BK + ch * 8;
      __builtin_amdgcn_global_load_lds((const GAS void*)src, (LAS void*)(base + (i * 256 + wid * 64) * 16), 16, 0, 0);
    }
    constexpr int CPR = BN / 8;
    constexpr int NCH = BK * CPR;
#pragma unroll
    for (int b = 0; b < NB; ++b) {
      const u16* Bm = b ? B2 : B;
#pragma unroll
      for (int i = 0; i < NCH / 256; ++i) {
        int c = tid + i * 256;
        int row = c / CPR, pos = c % CPR;
        int ch = pos ^ ((((row & 3) << 2) | ((row >> 2) & 3)) & (CPR - 1));
        const u16* src = Bm + (size_t)(kt * BK + row) * ldb + bn + ch * 8;
        __builtin_amdgcn_global_load_lds((const GAS void*)src,
                                         (LAS void*)(base + A_BYTES + b * B_BYTES + (i * 256 + wid * 64) * 16), 16, 0, 0);
      }
    }
  };

  const int nkt = K / BK;
  stage(0, 0);
  asm volatile("s_waitcnt vmcnt(0)" ::: "memory");
  __syncthreads();
  for (int kt = 0; kt < nkt; ++kt) {
    int cur = kt & 1;
    if (kt + 1 < nkt) stage(cur ^ 1, kt + 1);
    const char* base = lds + cur * STAGE;
#pragma unroll
    for (int kk = 0; kk < 2; ++kk) {
      bf16x8 af[MT];
#pragma unroll
      for (int i = 0; i < MT; ++i) {
        int row = wr * WM + i * 16 + (lane & 15);
        int ch = (kk * 4 + (lane >> 4)) ^ (row & 7);
        af[i] = *(const bf16x8*)(base + row * 128 + ch * 16);
      }
#pragma unroll
      for (int b = 0; b < NB; ++b) {
#pragma unroll
        for (int j = 0; j < NT; ++j) {
          bf16x8 bfr;
#pragma unroll
          for (int t = 0; t < 2; ++t) {
            int krow = kk * 32 + 8 * (lane >> 4) + 4 * t + ((lane & 15) >> 2);
            int col = wc * WN + j * 16 + 4 * (lane & 3);
            int ch = (col >> 3) ^ ((((krow & 3) << 2) | ((krow >> 2) & 3)) & (BN / 8 - 1));
            const u16* p = (const u16*)(base + A_BYTES + b * B_BYTES + krow * (BN * 2) + ch * 16 + (col & 7) * 2);
            s16x4 v = tr_read(p);
            bfr[4 * t + 0] = v[0]; bfr[4 * t + 1] = v[1]; bfr[4 * t + 2] = v[2]; bfr[4 * t + 3] = v[3];
          }
#pragma unroll
          for (int i = 0; i < MT; ++i)
            acc[b][i][j] = __builtin_amdgcn_mfma_f32_16x16x32_bf16(af[i], bfr, acc[b][i][j], 0, 0, 0);
        }
      }
    }
    asm volatile("s_waitcnt vmcnt(0)" ::: "memory");
    __syncthreads();
  }
#pragma unroll
  for (int i = 0; i < MT; ++i)
#pragma unroll
    for (int j = 0; j < NT; ++j)
#pragma unroll
      for (int r = 0; r < 4; ++r) {
        int row = bm + wr * WM + i * 16 + (lane >> 4) * 4 + r;
        int col = bn + wc * WN + j * 16 + (lane & 15);
        float v = acc[0][i][j][r];
        if (EPI == 0) {
          ((u16*)Cout)[(size_t)row * N + col] = f2bf(v);
        } else if (EPI == 1) {
          float xv = bf2f(((const u16*)resid)[(size_t)row * DM + col]);
          ((float*)Cout)[(size_t)row * DM + col] = xv + gvec[col] * v;
        } else if (EPI == 2) {
          float g = bf2f(f2bf(v)), u = bf2f(f2bf(acc[NB - 1][i][j][r]));
          g = g > 0.f ? g : 0.f;
          ((u16*)Cout)[(size_t)row * N + col] = f2bf(g * u);
        } else {
          float xv = ((const float*)resid)[(size_t)row * DM + col];
          ((u16*)Cout)[(size_t)row * DM + col] = f2bf(xv + gvec[col] * v);
        }
      }
}


__global__ __launch_bounds__(256) void k_xattn(const u16* __restrict__ qkv, const u16* __restrict__ kvm,
                                               u16* __restrict__ ox) {
  __shared__ __attribute__((aligned(16))) u16 sP[4][32][72];
  __shared__ __attribute__((aligned(16))) u16 sV[4][64][136];
  const int lane = threadIdx.x & 63, wid = threadIdx.x >> 6;
  const int pair = blockIdx.x * 4 + wid;
  const int seg = pair >> 5, h = pair & 31;
  const u16* qbase = qkv + (size_t)(seg * SEG) * QKV_LD + h * DH;
  const u16* kbase = qbase + HD;
  const u16* vbase = qbase + 2 * HD;
  const u16* kmb = kvm + ((size_t)seg * 2 + 0) * MEM * HD + h * DH;
  const u16* vmb = kvm + ((size_t)seg * 2 + 1) * MEM * HD + h * DH;
  for (int i = lane; i < 64 * 16; i += 64) {
    int key = i >> 4, c = (i & 15) * 8;
    bf16x8 v = {0, 0, 0, 0, 0, 0, 0, 0};
    if (key < 8) v = *(const bf16x8*)(vmb + (size_t)key * HD + c);
    else if (key < 40) v = *(const bf16x8*)(vbase + (size_t)(key - 8) * QKV_LD + c);
    *(bf16x8*)&sV[wid][key][c] = v;
  }
  f32x4 sc[2][3];
#pragma unroll
  for (int i = 0; i < 2; ++i)
#pragma unroll
    for (int j = 0; j < 3; ++j) sc[i][j] = f32x4{0, 0, 0, 0};
#pragma unroll
  for (int ks = 0; ks < 4; ++ks) {
    int kd = ks * 32 + (lane >> 4) * 8;
    bf16x8 a[2], b[3];
#pragma unroll
    for (int i = 0; i < 2; ++i) a[i] = *(const bf16x8*)(qbase + (size_t)(i * 16 + (lane & 15)) * QKV_LD + kd);
#pragma unroll
    for (int j = 0; j < 3; ++j) {
      int key = j * 16 + (lane & 15);
      if (key < 8) b[j] = *(const bf16x8*)(kmb + (size_t)key * HD + kd);
      else if (key < 40) b[j] = *(const bf16x8*)(kbase + (size_t)(key - 8) * QKV_LD + kd);
      else b[j] = bf16x8{0, 0, 0, 0, 0, 0, 0, 0};
    }
#pragma unroll
    for (int i = 0; i < 2; ++i)
#pragma unroll
      for (int j = 0; j < 3; ++j) sc[i][j] = __builtin_amdgcn_mfma_f32_16x16x32_bf16(a[i], b[j], sc[i][j], 0, 0, 0);
  }
  const float scale = 0.08838834764831845f;
#pragma unroll
  for (int i = 0; i < 2; ++i)
#pragma unroll
    for (int r = 0; r < 4; ++r) {
      int qrow = i * 16 + (lane >> 4) * 4 + r;
      float v[3];
      float mx = -3e38f;
#pragma unroll
      for (int j = 0; j < 3; ++j) {
        int key = j * 16 + (lane & 15);
        bool ok = (key < 8) || (key < 40 && (key - 8) <= qrow);
        v[j] = ok ? sc[i][j][r] * scale : -3e38f;
        mx = fmaxf(mx, v[j]);
      }
#pragma unroll
      for (int o = 1; o < 16; o <<= 1) mx = fmaxf(mx, __shfl_xor(mx, o));
      float sum = 0.f;
#pragma unroll
      for (int j = 0; j < 3; ++j) { v[j] = v[j] > -1e38f ? __expf(v[j] - mx) : 0.f; sum += v[j]; }
#pragma unroll
      for (int o = 1; o < 16; o <<= 1) sum += __shfl_xor(sum, o);
      float inv = 1.f / sum;
#pragma unroll
      for (int j = 0; j < 3; ++j) sP[wid][qrow][j * 16 + (lane & 15)] = f2bf(v[j] * inv);
      sP[wid][qrow][48 + (lane & 15)] = 0;
    }
  __syncthreads();
#pragma unroll
  for (int nt = 0; nt < 8; ++nt) {
    f32x4 o[2] = {f32x4{0, 0, 0, 0}, f32x4{0, 0, 0, 0}};
#pragma unroll
    for (int ks = 0; ks < 2; ++ks) {
      bf16x8 b;
#pragma unroll
      for (int t = 0; t < 2; ++t) {
        s16x4 v = tr_read(&sV[wid][ks * 32 + (lane >> 4) * 8 + 4 * t + ((lane & 15) >> 2)][nt * 16 + 4 * (lane & 3)]);
        b[4 * t + 0] = v[0]; b[4 * t + 1] = v[1]; b[4 * t + 2] = v[2]; b[4 * t + 3] = v[3];
      }
#pragma unroll
      for (int i = 0; i < 2; ++i) {
        bf16x8 a = *(const bf16x8*)&sP[wid][i * 16 + (lane & 15)][ks * 32 + (lane >> 4) * 8];
        o[i] = __builtin_amdgcn_mfma_f32_16x16x32_bf16(a, b, o[i], 0, 0, 0);
      }
    }
#pragma unroll
    for (int i = 0; i < 2; ++i)
#pragma unroll
      for (int r = 0; r < 4; ++r) {
        int qrow = i * 16 + (lane >> 4) * 4 + r;
        ox[(size_t)(seg * SEG + qrow) * HD + h * DH + nt * 16 + (lane & 15)] = f2bf(o[i][r]);
      }
  }
}

constexpr float FXS = 4294967296.f;
__global__ __launch_bounds__(256) void k_chain(const u16* __restrict__ mem0, const u16* __restrict__ Wq,
                                               const u16* __restrict__ Wk, const u16* __restrict__ Wv,
                                               const u16* __restrict__ Wo, const u16* __restrict__ qkv,
                                               u16* __restrict__ kvm, u64* gA, u64* accB) {
  __shared__ __attribute__((aligned(16))) u16 sMem[16][584];
  __shared__ __attribute__((aligned(16))) u16 sQ[16][136];
  __shared__ __attribute__((aligned(16))) u16 sK[48][136];
  __shared__ __attribute__((aligned(16))) u16 sV[64][136];
  __shared__ __attribute__((aligned(16))) u16 sP[16][72];
  __shared__ __attribute__((aligned(16))) u16 sO[16][136];
  const int tid = threadIdx.x, lane = tid & 63, wid = tid >> 6;
  const int w = blockIdx.x, h = w >> 3, j = w & 7;
  bf16x8 wf[18];
  if (wid < 3) {
    const u16* W = wid == 0 ? Wq : (wid == 1 ? Wk : Wv);
#pragma unroll
    for (int ks = 0; ks < 18; ++ks) {
#pragma unroll
      for (int e = 0; e < 8; ++e)
        wf[ks][e] = (short)W[(size_t)(ks * 32 + (lane >> 4) * 8 + e) * HD + h * DH + j * 16 + (lane & 15)];
    }
  }
  bf16x8 wo[2][4];
#pragma unroll
  for (int u = 0; u < 2; ++u) {
    int t = (u == 0) ? j + 8 * wid : j + 32;
    bool ok = (u == 0) || (wid == 0 && j < 4);
#pragma unroll
    for (int ks = 0; ks < 4; ++ks)
#pragma unroll
      for (int e = 0; e < 8; ++e)
        wo[u][ks][e] = ok ? (short)Wo[(size_t)(h * DH + ks * 32 + (lane >> 4) * 8 + e) * DM + t * 16 + (lane & 15)] : (short)0;
  }
  for (int i = tid; i < 16 * 584; i += 256) (&sMem[0][0])[i] = 0;
  for (int i = tid; i < 16 * 136; i += 256) { (&sQ[0][0])[i] = 0; (&sO[0][0])[i] = 0; }
  for (int i = tid; i < 64 * 136; i += 256) (&sV[0][0])[i] = 0;
  for (int i = tid; i < 48 * 136; i += 256) (&sK[0][0])[i] = 0;
  for (int i = tid; i < 16 * 72; i += 256) (&sP[0][0])[i] = 0;
  __syncthreads();
  for (int i = tid; i < MEM * DM; i += 256) sMem[i / DM][i % DM] = mem0[i];
  __syncthreads();
  for (int s = 0; s < NSEG; ++s) {
    const unsigned tag = s + 1;
    for (int i = tid; i < 32 * 16 * 2; i += 256) {
      int m = i >> 9, key = (i >> 4) & 31, c = (i & 15) * 8;
      bf16x8 v = *(const bf16x8*)(qkv + (size_t)(s * SEG + key) * QKV_LD + (1 + m) * HD + h * DH + c);
      if (m == 0) *(bf16x8*)&sK[8 + key][c] = v; else *(bf16x8*)&sV[8 + key][c] = v;
    }
    if (s > 0) {
      const u64* src = accB + (size_t)(s - 1) * (MEM * DM);
      u64 xs[18];
      for (;;) {
        bool ok = true;
#pragma unroll
        for (int i = 0; i < 18; ++i) xs[i] = __hip_atomic_load((GAS u64*)(src + tid + i * 256), RLX_AGENT);
#pragma unroll
        for (int i = 0; i < 18; ++i) ok &= (((xs[i] + (1ull << 55)) >> 56) == 32ull);
        if (ok) break;
        __builtin_amdgcn_s_sleep(1);
      }
#pragma unroll
      for (int i = 0; i < 18; ++i) {
        int e = tid + i * 256;
        long long v = (long long)(xs[i] - (32ull << 56));
        sMem[e / DM][e % DM] = f2bf((float)v * (1.f / FXS));
      }
    }
    __syncthreads();
    if (wid < 3) {
      f32x4 acc = {0, 0, 0, 0};
#pragma unroll
      for (int ks = 0; ks < 18; ++ks) {
        bf16x8 a = *(const bf16x8*)&sMem[lane & 15][ks * 32 + (lane >> 4) * 8];
        acc = __builtin_amdgcn_mfma_f32_16x16x32_bf16(a, wf[ks], acc, 0, 0, 0);
      }
      if (lane < 32) {
        u64* g = gA + (((size_t)h * 8 + j) * 3 + wid) * 64;
        __hip_atomic_store((GAS u64*)(g + lane), ((u64)tag << 32) | pack2(acc[0], acc[1]), RLX_AGENT);
        __hip_atomic_store((GAS u64*)(g + 32 + lane), ((u64)tag << 32) | pack2(acc[2], acc[3]), RLX_AGENT);
        if (wid > 0) {
          u16* d = kvm + ((size_t)s * 2 + (wid - 1)) * MEM * HD + h * DH + j * 16 + (lane & 15);
#pragma unroll
          for (int r = 0; r < 4; ++r) d[(size_t)((lane >> 4) * 4 + r) * HD] = f2bf(acc[r]);
        }
      }
    }
    {
      u64 xs[6];
      const u64* src = gA + (size_t)h * 8 * 3 * 64 + tid;
      for (;;) {
        bool ok = true;
#pragma unroll
        for (int i = 0; i < 6; ++i) xs[i] = __hip_atomic_load((GAS u64*)(src + i * 256), RLX_AGENT);
#pragma unroll
        for (int i = 0; i < 6; ++i) ok &= ((unsigned)(xs[i] >> 32) == tag);
        if (ok) break;
        __builtin_amdgcn_s_sleep(1);
      }
#pragma unroll
      for (int ii = 0; ii < 6; ++ii) {
        int i = tid + ii * 256;
        int jj = i / 192, mat = (i / 64) % 3, g = i & 63;
        u64 x = xs[ii];
        int l = g & 31, half = g >> 5;
        int col = jj * 16 + (l & 15), row = (l >> 4) * 4 + half * 2;
        u16 lo = (u16)x, hi = (u16)(x >> 16);
        if (mat == 0) { sQ[row][col] = lo; sQ[row + 1][col] = hi; }
        else if (mat == 1) { sK[row][col] = lo; sK[row + 1][col] = hi; }
        else { sV[row][col] = lo; sV[row + 1][col] = hi; }
      }
    }
    __syncthreads();
    if (wid == 0) {
      f32x4 sc[3] = {f32x4{0, 0, 0, 0}, f32x4{0, 0, 0, 0}, f32x4{0, 0, 0, 0}};
#pragma unroll
      for (int ks = 0; ks < 4; ++ks) {
        bf16x8 a = *(const bf16x8*)&sQ[lane & 15][ks * 32 + (lane >> 4) * 8];
#pragma unroll
        for (int jt = 0; jt < 3; ++jt) {
          bf16x8 b = *(const bf16x8*)&sK[jt * 16 + (lane & 15)][ks * 32 + (lane >> 4) * 8];
          sc[jt] = __builtin_amdgcn_mfma_f32_16x16x32_bf16(b, a, sc[jt], 0, 0, 0);
        }
      }
      const float scale = 0.08838834764831845f;
      float v[3][4], mx = -3e38f;
#pragma unroll
      for (int jt = 0; jt < 3; ++jt)
#pragma unroll
        for (int r = 0; r < 4; ++r) {
          int key = jt * 16 + (lane >> 4) * 4 + r;
          v[jt][r] = key < 40 ? sc[jt][r] * scale : -3e38f;
          mx = fmaxf(mx, v[jt][r]);
        }
      mx = fmaxf(mx, __shfl_xor(mx, 16));
      mx = fmaxf(mx, __shfl_xor(mx, 32));
      float sum = 0.f;
#pragma unroll
      for (int jt = 0; jt < 3; ++jt)
#pragma unroll
        for (int r = 0; r < 4; ++r) { v[jt][r] = v[jt][r] > -1e38f ? __expf(v[jt][r] - mx) : 0.f; sum += v[jt][r]; }
      sum += __shfl_xor(sum, 16);
      sum += __shfl_xor(sum, 32);
      float inv = 1.f / sum;
      if ((lane & 15) < 8) {
#pragma unroll
        for (int jt = 0; jt < 3; ++jt) {
          unsigned lo = pack2(v[jt][0] * inv, v[jt][1] * inv), hi = pack2(v[jt][2] * inv, v[jt][3] * inv);
          *(uint2*)&sP[lane & 15][jt * 16 + (lane >> 4) * 4] = make_uint2(lo, hi);
        }
      }
    }
    __syncthreads();
#pragma unroll
    for (int u = 0; u < 2; ++u) {
      int nt = wid * 2 + u;
      f32x4 o = {0, 0, 0, 0};
#pragma unroll
      for (int ks = 0; ks < 2; ++ks) {
        bf16x8 b;
#pragma unroll
        for (int t = 0; t < 2; ++t) {
          s16x4 v = tr_read(&sV[ks * 32 + (lane >> 4) * 8 + 4 * t + ((lane & 15) >> 2)][nt * 16 + 4 * (lane & 3)]);
          b[4 * t + 0] = v[0]; b[4 * t + 1] = v[1]; b[4 * t + 2] = v[2]; b[4 * t + 3] = v[3];
        }
        bf16x8 a = *(const bf16x8*)&sP[lane & 15][ks * 32 + (lane >> 4) * 8];
        o = __builtin_amdgcn_mfma_f32_16x16x32_bf16(a, b, o, 0, 0, 0);
      }
      if (lane < 32) {
#pragma unroll
        for (int r = 0; r < 4; ++r) sO[(lane >> 4) * 4 + r][nt * 16 + (lane & 15)] = f2bf(o[r]);
      }
    }
    __syncthreads();
    if (s + 1 < NSEG) {
#pragma unroll
      for (int u = 0; u < 2; ++u) {
        if (u == 1 && !(wid == 0 && j < 4)) break;
        int t = (u == 0) ? j + 8 * wid : j + 32;
        f32x4 acc = {0, 0, 0, 0};
#pragma unroll
        for (int ks = 0; ks < 4; ++ks) {
          bf16x8 a = *(const bf16x8*)&sO[lane & 15][ks * 32 + (lane >> 4) * 8];
          acc = __builtin_amdgcn_mfma_f32_16x16x32_bf16(a, wo[u][ks], acc, 0, 0, 0);
        }
        if (lane < 32) {
          u64* dst = accB + (size_t)s * (MEM * DM) + t * 16 + (lane & 15);
#pragma unroll
          for (int r = 0; r < 4; ++r) {
            long long fx = (long long)(acc[r] * FXS);
            __hip_atomic_fetch_add((GAS u64*)(dst + (size_t)((lane >> 4) * 4 + r) * DM), (1ull << 56) + (u64)fx, RLX_AGENT);
          }
        }
      }
    }
    __syncthreads();
  }
}

extern "C" void kernel_launch(void* const* d_in, const int* in_sizes, int n_in, void* d_out, int out_size, void* d_ws,
                              size_t ws_size, hipStream_t stream) {
  const u16* x = (const u16*)d_in[0];
  const u16* mem0 = (const u16*)d_in[1];
  const u16* Wq = (const u16*)d_in[2];
  const u16* Wk = (const u16*)d_in[3];
  const u16* Wv = (const u16*)d_in[4];
  const u16* Wo = (const u16*)d_in[5];
  const u16* Wg = (const u16*)d_in[8];
  const u16* Wu = (const u16*)d_in[9];
  const u16* Wd = (const u16*)d_in[10];
  const u16* c = (const u16*)d_in[11];
  const u16* wmod = (const u16*)d_in[12];
  char* ws = (char*)d_ws;
  u64* accB = (u64*)ws;
  u64* gA = (u64*)(ws + 2359296);
  size_t zbytes = 2359296 + 393216;
  char* p = ws + zbytes;
  float* mod = (float*)p; p += 6 * DM * 4 + 256;
  u16* h1 = (u16*)p; p += (size_t)S_ * DM * 2;
  u16* qkv = (u16*)p; p += (size_t)S_ * QKV_LD * 2;
  u16* kvm = (u16*)p; p += (size_t)NSEG * 2 * MEM * HD * 2;
  u16* ox = (u16*)p; p += (size_t)S_ * HD * 2;
  float* x1 = (float*)p; p += (size_t)S_ * DM * 4;
  u16* h2 = (u16*)p; p += (size_t)S_ * DM * 2;
  u16* hff = (u16*)p; p += (size_t)S_ * FF * 2;
  hipMemsetAsync(ws, 0, zbytes, stream);
  k_mod<<<6 * DM / 64, 1024, 0, stream>>>(c, wmod, mod);
  k_ln<false><<<S_ / 4, 256, 0, stream>>>(x, mod + 0 * DM, mod + 1 * DM, h1);
  k_gemm<128, 128, 0><<<dim3(HD / 128, S_ / 128), 256, 0, stream>>>(h1, Wq, nullptr, DM, HD, QKV_LD, qkv, nullptr, nullptr);
  k_gemm<128, 128, 0><<<dim3(HD / 128, S_ / 128), 256, 0, stream>>>(h1, Wk, nullptr, DM, HD, QKV_LD, qkv + HD, nullptr, nullptr);
  k_gemm<128, 128, 0><<<dim3(HD / 128, S_ / 128), 256, 0, stream>>>(h1, Wv, nullptr, DM, HD, QKV_LD, qkv + 2 * HD, nullptr, nullptr);
  k_chain<<<256, 256, 0, stream>>>(mem0, Wq, Wk, Wv, Wo, qkv, kvm, gA, accB);
  k_xattn<<<NSEG * NH / 4, 256, 0, stream>>>(qkv, kvm, ox);
  k_gemm<64, 64, 1><<<dim3(DM / 64, S_ / 64), 256, 0, stream>>>(ox, Wo, nullptr, HD, DM, DM, x1, x, mod + 2 * DM);
  k_ln<true><<<S_ / 4, 256, 0, stream>>>(x1, mod + 3 * DM, mod + 4 * DM, h2);
  k_gemm<128, 64, 2><<<dim3(FF / 64, S_ / 128), 256, 0, stream>>>(h2, Wg, Wu, DM, FF, FF, hff, nullptr, nullptr);
  k_gemm<64, 64, 3><<<dim3(DM / 64, S_ / 64), 256, 0, stream>>>(hff, Wd, nullptr, FF, DM, DM, d_out, x1, mod + 5 * DM);
}
```
